```python
import torch

def _softmax(x, axis=-1):
    return torch.softmax(x, dim=axis)
def _gelu(x):
    return torch.nn.functional.gelu(x, approximate="tanh")
def _silu(x):
    return torch.nn.functional.silu(x)
def _sigmoid(x):
    return torch.sigmoid(x)
def _layernorm(x, axis=-1, eps=1e-5):
    m = x.mean(dim=axis, keepdim=True)
    v = x.var(dim=axis, keepdim=True, unbiased=False)
    return (x - m) / torch.sqrt(v + eps)
def _scatter_add(tgt, idx, src, axis=0):
    return tgt.clone().index_add_(axis, idx, src)
def _segment_sum(src, seg, n):
    out = torch.zeros((n,) + src.shape[1:], dtype=src.dtype)
    return out.index_add_(0, seg, src)
def _pad(x, pw):
    flat = tuple(v for pair in reversed(pw) for v in pair)
    return torch.nn.functional.pad(x, flat)

kernel_name = 'recurrent_scan__mha_block_cached__glu_gated_reglu__decode'

def setup_inputs():

    _w = (lambda a: (lambda t: t.to(torch.bfloat16) if t.is_floating_point() else t)(torch.as_tensor(a)))
    def _A_setup():
        B, Sq, D = 1, 1, 640
        H, Sk, Dh = 32, 32768, 128
        return {
            "x": _w((lambda *s: torch.randn(*s, dtype=torch.bfloat16) * 0.4)(B, Sq, D)),
            "k_cache": _w((lambda *s: torch.randn(*s, dtype=torch.bfloat16) * 1.6)(B, H, Sk, Dh)),
            "v_cache": _w((lambda *s: torch.randn(*s, dtype=torch.bfloat16) * 1.6)(B, H, Sk, Dh)),
            "wq": _w((lambda *s: torch.randn(*s, dtype=torch.bfloat16) * 1.6)(D, H * Dh) * (D ** -0.5)),
            "wo": _w((lambda *s: torch.randn(*s, dtype=torch.bfloat16) * 1.6)(H * Dh, D) * ((H * Dh) ** -0.5)),
        }
    _A_in = _A_setup()

    def _B_setup():
        B, S, D, F = 1, 1, 640, 1728
        return {
            "x": _w((lambda *s: torch.randn(*s, dtype=torch.bfloat16) * 0.4)(B, S, D)),
            "W_gate": _w((lambda *s: torch.randn(*s, dtype=torch.bfloat16) * 1.6)(D, F) * (2.0 / D) ** 0.5),
            "W_up": _w((lambda *s: torch.randn(*s, dtype=torch.bfloat16) * 1.6)(D, F) * (2.0 / D) ** 0.5),
            "W_down": _w((lambda *s: torch.randn(*s, dtype=torch.bfloat16) * 1.6)(F, D) * (1.0 / F) ** 0.5),
        }
    _B_in = _B_setup()

    return {
        "A_x": _A_in["x"],
        "A_k_cache": _A_in["k_cache"],
        "A_v_cache": _A_in["v_cache"],
        "A_wq": _A_in["wq"],
        "A_wo": _A_in["wo"],
        "B_x": _B_in["x"],
        "B_W_gate": _B_in["W_gate"],
        "B_W_up": _B_in["W_up"],
        "B_W_down": _B_in["W_down"],
    }

def reference(A_x, A_k_cache, A_v_cache, A_wq, A_wo, B_x, B_W_gate, B_W_up, B_W_down):
    def _A_ref(x, k_cache, v_cache, wq, wo):
        B, Sq, D = x.shape
        H, Dh = 32, 128
        q = (lambda x, *d: x.permute(*d))((x @ wq).reshape(B, Sq, H, Dh), 0, 2, 1, 3)
        scores = torch.einsum("bhqd,bhkd->bhqk", q, k_cache) * (Dh ** -0.5)
        p = _softmax(scores, axis=-1)
        o = torch.einsum("bhqk,bhkd->bhqd", p, v_cache)
        return (lambda x, *d: x.permute(*d))(o, 0, 2, 1, 3).reshape(B, Sq, H * Dh) @ wo

    def _B_ref(x, W_gate, W_up, W_down):
        gate = torch.einsum("bsd,df->bsf", x, W_gate)
        up = torch.einsum("bsd,df->bsf", x, W_up)
        gate = gate * (lambda x, dt: x.to(dt))(gate > 0, gate.dtype)
        h = gate * up
        return torch.einsum("bsf,fd->bsd", h, W_down)

    x = A_x
    xn = _layernorm(x, axis=-1)
    s = _A_ref(xn, A_k_cache, A_v_cache, A_wq, A_wo)
    g = _silu(_B_ref(xn, B_W_gate, B_W_up, B_W_down))
    return x + s * g
```

```cpp
#include <hip/hip_runtime.h>
#include <hip/hip_bf16.h>
#include <stdint.h>

typedef unsigned u32;
typedef unsigned u32x4 __attribute__((ext_vector_type(4)));
typedef float f32x4 __attribute__((ext_vector_type(4)));
typedef __bf16 bf16x2 __attribute__((ext_vector_type(2)));

constexpr int D = 640, H = 32, SK = 32768, DH = 128, F = 1728;
constexpr int NSPLIT = 16;
constexpr int KEYS_PER_WG = SK / NSPLIT;

__device__ __forceinline__ float bf2f(unsigned short b) { return __uint_as_float((u32)b << 16); }
__device__ __forceinline__ float rbf(float x) { return __bfloat162float(__float2bfloat16(x)); }
__device__ __forceinline__ float dot2(u32 a, u32 b, float c) {
  return __builtin_amdgcn_fdot2_f32_bf16(__builtin_bit_cast(bf16x2, a), __builtin_bit_cast(bf16x2, b), c, false);
}
template <int CTRL> __device__ __forceinline__ float dpp(float x) {
  return __builtin_bit_cast(float, __builtin_amdgcn_mov_dpp(__builtin_bit_cast(int, x), CTRL, 0xf, 0xf, true));
}
constexpr int XOR1 = 0xB1, XOR2 = 0x4E, XOR7 = 0x141, XOR8 = 0x128, ROR4 = 0x124;

__device__ __forceinline__ void layernorm_lds(const unsigned short* x, float* xn, float* red) {
  int t = threadIdx.x;
  float v[3]; float s = 0.f;
  for (int i = 0; i < 3; ++i) { int k = t + 256 * i; v[i] = k < D ? bf2f(x[k]) : 0.f; s += v[i]; }
  for (int m = 32; m >= 1; m >>= 1) s += __shfl_xor(s, m);
  if ((t & 63) == 0) red[t >> 6] = s;
  __syncthreads();
  float mean = (red[0] + red[1] + red[2] + red[3]) * (1.f / D);
  __syncthreads();
  float q = 0.f;
  for (int i = 0; i < 3; ++i) { int k = t + 256 * i; float d = k < D ? v[i] - mean : 0.f; q += d * d; }
  for (int m = 32; m >= 1; m >>= 1) q += __shfl_xor(q, m);
  if ((t & 63) == 0) red[t >> 6] = q;
  __syncthreads();
  float var = (red[0] + red[1] + red[2] + red[3]) * (1.f / D);
  float mb = rbf(mean), vb = rbf(var);
  float den = rbf(sqrtf(rbf(vb + 1e-5f)));
  for (int i = 0; i < 3; ++i) { int k = t + 256 * i; if (k < D) xn[k] = rbf(rbf(v[i] - mb) / den); }
  __syncthreads();
}

__global__ void __launch_bounds__(256) k_prep(const unsigned short* x, const unsigned short* wq, const unsigned short* wg, const unsigned short* wu,
                                              unsigned short* qout, float* hout) {
  __shared__ float xn[D]; __shared__ float red[4]; __shared__ float part[2][64][33];
  layernorm_lds(x, xn, red);
  int t = threadIdx.x, c4 = t & 3, r = t >> 2;
  int b = blockIdx.x;
  bool isq = b < 128;
  int ld = isq ? H * DH : F;
  int col0 = isq ? b * 32 : (b - 128) * 32;
  for (int mtx = 0; mtx < (isq ? 1 : 2); ++mtx) {
    const unsigned short* w = isq ? wq : (mtx == 0 ? wg : wu);
    float acc[8] = {0, 0, 0, 0, 0, 0, 0, 0};
    for (int k = r; k < D; k += 64) {
      u32x4 v = *(const u32x4*)(w + (size_t)k * ld + col0 + c4 * 8);
      float xv = xn[k];
      for (int j = 0; j < 4; ++j) { acc[2 * j] += xv * __uint_as_float(v[j] << 16); acc[2 * j + 1] += xv * __uint_as_float(v[j] & 0xffff0000u); }
    }
    for (int j = 0; j < 8; ++j) part[mtx][r][c4 * 8 + j] = acc[j];
  }
  __syncthreads();
  if (t < 32) {
    float s0 = 0.f, s1 = 0.f;
    for (int i = 0; i < 64; ++i) { s0 += part[0][i][t]; if (!isq) s1 += part[1][i][t]; }
    if (isq) { qout[col0 + t] = __builtin_bit_cast(unsigned short, __float2bfloat16(s0)); }
    else { float g = rbf(s0), u = rbf(s1); g = g > 0.f ? g : g * 0.f; hout[col0 + t] = rbf(g * u); }
  }
}

__global__ void __launch_bounds__(256) k_attn(const unsigned short* q, const unsigned short* K, const unsigned short* V, float* pml, float* po) {
  __shared__ float mrg[4][132];
  int s = blockIdx.x % NSPLIT, h = blockIdx.x / NSPLIT;
  int tid = threadIdx.x, lane = tid & 63, wave = __builtin_amdgcn_readfirstlane(tid >> 6), r = lane >> 4, c = lane & 15;
  u32x4 qv = *(const u32x4*)(q + h * DH + 8 * c);
  const size_t base = ((size_t)h * SK + (size_t)s * KEYS_PER_WG) * DH + (size_t)r * DH + 8 * c;
  constexpr int TPW = KEYS_PER_WG / 16 / 4;
  const unsigned short* kp = K + base + (size_t)wave * 16 * DH;
  const unsigned short* vp = V + base + (size_t)wave * 16 * DH;
  u32x4 Kn[4], Vn[4];
  for (int t = 0; t < 4; ++t) { Kn[t] = __builtin_nontemporal_load((const u32x4*)(kp + t * 4 * DH)); Vn[t] = __builtin_nontemporal_load((const u32x4*)(vp + t * 4 * DH)); }
  float m = -INFINITY, l = 0.f, acc[8] = {0, 0, 0, 0, 0, 0, 0, 0};
  constexpr float SC = 0.08838834764831845f, L2E = 1.4426950408889634f;
  for (int i = 0; i < TPW; ++i) {
    u32x4 Kc[4], Vc[4];
    for (int t = 0; t < 4; ++t) { Kc[t] = Kn[t]; Vc[t] = Vn[t]; }
    if (i + 1 < TPW) {
      kp += 64 * DH; vp += 64 * DH;
      for (int t = 0; t < 4; ++t) { Kn[t] = __builtin_nontemporal_load((const u32x4*)(kp + t * 4 * DH)); Vn[t] = __builtin_nontemporal_load((const u32x4*)(vp + t * 4 * DH)); }
    }
    float x[4];
    for (int t = 0; t < 4; ++t) { float d = 0.f; for (int k = 0; k < 4; ++k) d = dot2(Kc[t][k], qv[k], d);
      d += dpp<XOR8>(d); d += dpp<XOR7>(d); d += dpp<XOR2>(d); d += dpp<XOR1>(d);
      x[t] = rbf(rbf(d) * SC) * L2E; }
    float pm = fmaxf(fmaxf(x[0], x[1]), fmaxf(x[2], x[3]));
    float mn = fmaxf(m, pm);
    float alpha = __builtin_amdgcn_exp2f(m - mn);
    m = mn; l *= alpha;
    for (int k = 0; k < 8; ++k) acc[k] *= alpha;
    for (int t = 0; t < 4; ++t) {
      float p = __builtin_amdgcn_exp2f(x[t] - mn); l += p;
      for (int k = 0; k < 4; ++k) { acc[2 * k] += p * __uint_as_float(Vc[t][k] << 16); acc[2 * k + 1] += p * __uint_as_float(Vc[t][k] & 0xffff0000u); }
    }
  }
  float M = fmaxf(m, __shfl_xor(m, 16)); M = fmaxf(M, __shfl_xor(M, 32));
  float w = __builtin_amdgcn_exp2f(m - M);
  l *= w; l += __shfl_xor(l, 16); l += __shfl_xor(l, 32);
  for (int k = 0; k < 8; ++k) { float a = acc[k] * w; a += __shfl_xor(a, 16); a += __shfl_xor(a, 32); acc[k] = a; }
  if (r == 0) { for (int k = 0; k < 8; ++k) mrg[wave][4 + 8 * c + k] = acc[k]; if (c == 0) { mrg[wave][0] = M; mrg[wave][1] = l; } }
  __syncthreads();
  if (tid < 128) {
    float MM = fmaxf(fmaxf(mrg[0][0], mrg[1][0]), fmaxf(mrg[2][0], mrg[3][0]));
    float L = 0.f, o = 0.f;
    for (int wv = 0; wv < 4; ++wv) { float ww = __builtin_amdgcn_exp2f(mrg[wv][0] - MM); L += ww * mrg[wv][1]; o += ww * mrg[wv][4 + tid]; }
    po[(size_t)blockIdx.x * DH + tid] = o;
    if (tid == 0) { pml[2 * blockIdx.x] = MM; pml[2 * blockIdx.x + 1] = L; }
  }
}

__global__ void __launch_bounds__(320) k_fin1(const float* pml, const float* po, const float* hin, const unsigned short* wo, const unsigned short* wd, float* part) {
  __shared__ float o[128]; __shared__ float red[4][D];
  int b = blockIdx.x, t = threadIdx.x;
  int nrows; const unsigned short* w;
  if (b < H) {
    if (t < 128) {
      float M = -INFINITY;
      for (int s = 0; s < NSPLIT; ++s) M = fmaxf(M, pml[2 * (b * NSPLIT + s)]);
      float L = 0.f, a = 0.f;
      for (int s = 0; s < NSPLIT; ++s) { float ww = __builtin_amdgcn_exp2f(pml[2 * (b * NSPLIT + s)] - M); L += ww * pml[2 * (b * NSPLIT + s) + 1]; a += ww * po[(size_t)(b * NSPLIT + s) * DH + t]; }
      o[t] = rbf(a / L);
    }
    nrows = 128; w = wo + (size_t)b * 128 * D;
  } else {
    if (t < 64) o[t] = hin[(b - H) * 64 + t];
    nrows = 64; w = wd + (size_t)(b - H) * 64 * D;
  }
  __syncthreads();
  int c8 = t % 80, rg = t / 80;
  float acc[8] = {0, 0, 0, 0, 0, 0, 0, 0};
  for (int k = rg; k < nrows; k += 4) {
    u32x4 v = *(const u32x4*)(w + (size_t)k * D + c8 * 8);
    float xv = o[k];
    for (int j = 0; j < 4; ++j) { acc[2 * j] += xv * __uint_as_float(v[j] << 16); acc[2 * j + 1] += xv * __uint_as_float(v[j] & 0xffff0000u); }
  }
  for (int j = 0; j < 8; ++j) red[rg][c8 * 8 + j] = acc[j];
  __syncthreads();
  for (int c = t; c < D; c += 320) part[(size_t)b * D + c] = red[0][c] + red[1][c] + red[2][c] + red[3][c];
}

__global__ void __launch_bounds__(64) k_fin2(const float* part, const unsigned short* x, unsigned short* out) {
  int c = blockIdx.x * 64 + threadIdx.x;
  float s = 0.f, f = 0.f;
  for (int i = 0; i < H; ++i) s += part[(size_t)i * D + c];
  for (int i = 0; i < 27; ++i) f += part[(size_t)(H + i) * D + c];
  s = rbf(s); f = rbf(f);
  float g = rbf(f / (1.f + __expf(-f)));
  float r = bf2f(x[c]) + rbf(s * g);
  out[c] = __builtin_bit_cast(unsigned short, __float2bfloat16(r));
}

extern "C" void kernel_launch(void* const* d_in, const int* in_sizes, int n_in,
                              void* d_out, int out_size, void* d_ws, size_t ws_size,
                              hipStream_t stream) {
  const unsigned short* x = (const unsigned short*)d_in[0];
  const unsigned short* K = (const unsigned short*)d_in[1];
  const unsigned short* V = (const unsigned short*)d_in[2];
  const unsigned short* wq = (const unsigned short*)d_in[3];
  const unsigned short* wo = (const unsigned short*)d_in[4];
  const unsigned short* wg = (const unsigned short*)d_in[6];
  const unsigned short* wu = (const unsigned short*)d_in[7];
  const unsigned short* wd = (const unsigned short*)d_in[8];
  char* ws = (char*)d_ws;
  unsigned short* q = (unsigned short*)ws;
  float* hbuf = (float*)(ws + 8192);
  float* pml = (float*)(ws + 16384);
  float* po = (float*)(ws + 32768);
  float* part = (float*)(ws + 32768 + 262144);
  k_prep<<<128 + 54, 256, 0, stream>>>(x, wq, wg, wu, q, hbuf);
  k_attn<<<H * NSPLIT, 256, 0, stream>>>(q, K, V, pml, po);
  k_fin1<<<H + 27, 320, 0, stream>>>(pml, po, hbuf, wo, wd, part);
  k_fin2<<<10, 64, 0, stream>>>(part, x, (unsigned short*)d_out);
}
```

```cpp
#include <hip/hip_runtime.h>
#include <hip/hip_bf16.h>
#include <stdint.h>

typedef unsigned u32;
typedef unsigned long long u64;
typedef unsigned u32x2 __attribute__((ext_vector_type(2)));
typedef unsigned u32x4 __attribute__((ext_vector_type(4)));
typedef unsigned long long u64x2 __attribute__((ext_vector_type(2)));
typedef unsigned long long u64x8 __attribute__((ext_vector_type(8)));
typedef float f32x2 __attribute__((ext_vector_type(2)));
typedef float f32x4 __attribute__((ext_vector_type(4)));
typedef __bf16 bf16x2 __attribute__((ext_vector_type(2)));
typedef __bf16 bf16x8 __attribute__((ext_vector_type(8)));
typedef __amdgpu_buffer_rsrc_t rsrc_t;
#define LAS __attribute__((address_space(3)))
#define GAS __attribute__((address_space(1)))

constexpr int D = 640, H = 32, SK = 32768, DH = 128, F = 1728;
constexpr int NWG = 256, NTHR = 320;
constexpr int NFFN = 216;
constexpr int NRED = 80, RC = 8;
constexpr int NT = 64;
constexpr u32 KV_BYTES = (u32)H * SK * DH * 2;
constexpr float SCALE = 0.08838834764831845f, L2E = 1.4426950408889634f;
constexpr int NTL = 2, SC1 = 16, SC1_NT = 18;
constexpr u32 SPIN_CAP = 400000;
constexpr u32 CODE_SPAN = 24200;

constexpr u32 T_FIRST = 140;
constexpr u32 N_A = 24, PER_A = 15400;
constexpr u32 PER_B = 13640;
constexpr u32 T_START = 250, T_START_LOOK = 630;
constexpr u32 N_CHK = 30, T_CHK_LOOK = 160, T_CHK_MIN = 60;
constexpr int CHK_RANK = 4;
constexpr u32 T_Q = 130, T_WD = 60, T_H = 450, T_FF = 1400, T_ML = 500;
constexpr u32 T_WO = 15, T_WO_SPREAD = 45, T_HOP1 = 125, T_HOP2 = 240;

constexpr u32 WS_EPOCH = 0;
constexpr u32 WS_T0 = 1024;
constexpr u32 WS_TB = 2048;
constexpr u32 WS_QP = 4096;
constexpr u32 WS_ML = WS_QP + NWG * 1024;
constexpr u32 WS_IN1 = WS_ML + NWG * 16;
constexpr u32 WS_IN2 = WS_IN1 + NWG * 1024;
constexpr u32 WS_FF = WS_IN2 + NWG * D * 8;
constexpr u32 WS_GU = WS_FF + 32 * D * 8;
constexpr u32 WS_H = WS_GU + NFFN * 1024;
constexpr u32 WS_CK = WS_H + 32 * 512;
constexpr u32 WS_END = WS_CK + 8 * 512;

constexpr u32 L_FLAG = 0;
constexpr u32 L_TAG = 16;
constexpr u32 L_M = 32;
constexpr u32 L_F = 48;
constexpr u32 L_Q = 64;
constexpr u32 L_G = L_Q + 256;
constexpr u32 L_RS = L_G + 64;
constexpr u32 L_O = L_RS + 128;
constexpr u32 L_LATE = L_O + 32;
constexpr u32 L_HB = 1024;
constexpr u32 L_REC = 2048;
constexpr u32 L_S = L_REC + 4096;
constexpr u32 L_WO = L_S + 16384;
constexpr u32 LDS_BYTES = 96 * 1024;
static_assert(L_WO + 20480 <= LDS_BYTES, "LDS map");

__device__ __forceinline__ float rbf(float x) { return (float)(__bf16)x; }
__device__ __forceinline__ u32 pack2(float lo, float hi) { bf16x2 v = {(__bf16)lo, (__bf16)hi}; return __builtin_bit_cast(u32, v); }
__device__ __forceinline__ float f_lo(u32 v) { return __uint_as_float(v << 16); }
__device__ __forceinline__ float f_hi(u32 v) { return __uint_as_float(v & 0xffff0000u); }
__device__ __forceinline__ float ex2(float d) { return __builtin_amdgcn_exp2f(d * L2E); }
__device__ __forceinline__ float finite_or_0(float m) { return m == -INFINITY ? 0.f : m; }
__device__ __forceinline__ float dot2(u32 a, u32 b, float c) {
  return __builtin_amdgcn_fdot2_f32_bf16(__builtin_bit_cast(bf16x2, a), __builtin_bit_cast(bf16x2, b), c, false);
}
template <int CTRL> __device__ __forceinline__ u32 dppu(u32 x) { return (u32)__builtin_amdgcn_mov_dpp((int)x, CTRL, 0xf, 0xf, true); }
template <int CTRL> __device__ __forceinline__ float dpp(float x) { return __uint_as_float(dppu<CTRL>(__float_as_uint(x))); }
constexpr int XOR1 = 0xB1, XOR2 = 0x4E, XOR7 = 0x141, XOR8 = 0x128, ROR4 = 0x124, BCAST = 0x150;
__device__ __forceinline__ f32x2 pair16(float x) { auto s = __builtin_amdgcn_permlane16_swap(__float_as_uint(x), __float_as_uint(x), false, false); return f32x2{__uint_as_float(s[0]), __uint_as_float(s[1])}; }
__device__ __forceinline__ f32x2 pair32(float x) { auto s = __builtin_amdgcn_permlane32_swap(__float_as_uint(x), __float_as_uint(x), false, false); return f32x2{__uint_as_float(s[0]), __uint_as_float(s[1])}; }
__device__ __forceinline__ float sum_hi3(float x) { x += dpp<XOR8>(x); f32x2 p = pair16(x); p = pair32(p.x + p.y); return p.x + p.y; }
__device__ __forceinline__ float max_hi3(float x) { x = fmaxf(x, dpp<XOR8>(x)); f32x2 p = pair16(x); p = pair32(fmaxf(p.x, p.y)); return fmaxf(p.x, p.y); }
__device__ __forceinline__ float sum_hi4(float x) { return sum_hi3(x + dpp<ROR4>(x)); }
__device__ __forceinline__ float wave_sum(float x) { x += dpp<XOR1>(x); x += dpp<XOR2>(x); x += dpp<XOR7>(x); return sum_hi3(x); }
__device__ __forceinline__ float wave_max(float x) { x = fmaxf(x, dpp<XOR1>(x)); x = fmaxf(x, dpp<XOR2>(x)); x = fmaxf(x, dpp<XOR7>(x)); return max_hi3(x); }
template <int CTRL> __device__ __forceinline__ float comb(float lo, float hi, bool sel) { const float a = lo + dpp<CTRL>(lo), b = hi + dpp<CTRL>(hi); return sel ? b : a; }
__device__ __forceinline__ f32x4 rows4(u32 ww, const u32 (&msk)[4], u32x4 rows, f32x4 acc) {
  const u32x4 a = {ww & msk[0], ww & msk[1], ww & msk[2], ww & msk[3]};
  return __builtin_amdgcn_mfma_f32_16x16x32_bf16(__builtin_bit_cast(bf16x8, a), __builtin_bit_cast(bf16x8, rows), acc, 0, 0, 0);
}
__device__ __forceinline__ rsrc_t mkrs(const void* p, u32 bytes) { return __builtin_amdgcn_make_buffer_rsrc((void*)p, 0, (int)bytes, 0x00020000); }
__device__ __forceinline__ void dma16(const void* p, u32 bytes, u32 lds_off, u32 voff, u32 soff) {
  const u64 a = (u64)p; const u32x4 rs = {(u32)a, (u32)(a >> 32) & 0xffffu, bytes, 0x00020000u}; u32 keep;
  asm volatile("s_mov_b32 %0, m0\n\ts_mov_b32 m0, %1\n\ts_nop 0\n\tbuffer_load_dwordx4 %2, %3, %4 offen nt lds\n\ts_mov_b32 m0, %0"
               : "=&s"(keep) : "s"(lds_off), "v"(voff), "s"(rs), "s"(soff) : "memory");
}
__device__ __forceinline__ u32 now() { return (u32)__builtin_amdgcn_s_memrealtime(); }
__device__ __forceinline__ void until(u32 t) { while ((int)(now() - t) < 0) __builtin_amdgcn_s_sleep(1); }
__device__ __forceinline__ u32 until_late(u32 due_) { u32 t = now(); while ((int)(t - due_) < 0) { __builtin_amdgcn_s_sleep(1); t = now(); } return t - due_; }
__device__ __forceinline__ void until_spin(u32 t) { while ((int)(now() - t) < 0) {} }
__device__ __forceinline__ u32 due(u32 n) { return T_FIRST + (n <= N_A ? n * PER_A : N_A * PER_A + (n - N_A) * PER_B) / 256u; }

#define LD16(rs, voff, soff, aux) __builtin_amdgcn_raw_buffer_load_b128((rs), (voff), (soff), (aux))
#define LD8(rs, voff, soff, aux) __builtin_amdgcn_raw_buffer_load_b64((rs), (voff), (soff), (aux))
#define LD4(rs, voff, soff, aux) __builtin_amdgcn_raw_buffer_load_b32((rs), (voff), (soff), (aux))
#define ST16(v, rs, voff, soff) __builtin_amdgcn_raw_buffer_store_b128((v), (rs), (voff), (soff), SC1)
#define ST8(v, rs, voff, soff) __builtin_amdgcn_raw_buffer_store_b64((v), (rs), (voff), (soff), SC1)
#define SB() __builtin_amdgcn_sched_barrier(0)
#define FENCE() asm volatile("" ::: "memory")
#define WAIT_LDS() asm volatile("s_waitcnt lgkmcnt(0)" ::: "memory")
#define BARRIER() do { WAIT_LDS(); __builtin_amdgcn_s_barrier(); FENCE(); } while (0)
#define FLAG(i) (*(volatile LAS u32*)(L + L_FLAG + 4 * (i)))
#define WAIT_FLAG(i, v, slp) do { for (u32 sp_ = 0; FLAG(i) < (v) && sp_ < 64u * SPIN_CAP; ++sp_) __builtin_amdgcn_s_sleep(slp); FENCE(); } while (0)
#define POST_FLAG(i) do { WAIT_LDS(); if (lane == 0) __hip_atomic_fetch_add((LAS u32*)(L + L_FLAG + 4 * (i)), 1u, __ATOMIC_RELAXED, __HIP_MEMORY_SCOPE_WORKGROUP); } while (0)

struct Tag { u32 hi, lo; };
__device__ __forceinline__ u32x4 enc2(float a, float b, Tag t) {
  return u32x4{((__float_as_uint(a) + 0x80u) >> 8) | t.lo, t.hi, ((__float_as_uint(b) + 0x80u) >> 8) | t.lo, t.hi};
}
__device__ __forceinline__ float dec(u32 lo) { return __uint_as_float(lo << 8); }
__device__ __forceinline__ bool ok1(u32x2 g, Tag t) { return (((g.x ^ t.lo) >> 24) | (g.y ^ t.hi)) == 0u; }
__device__ __forceinline__ bool ok2(u32x4 g, Tag t) { return ((((g.x ^ t.lo) | (g.z ^ t.lo)) >> 24) | (g.y ^ t.hi) | (g.w ^ t.hi)) == 0u; }
__device__ __forceinline__ int ticks_since(u32 granule_lo, u32 t) { return (int)((granule_lo - t) << 8) >> 8; }
template <int N, u32 STEP, int SLEEP, int AUX> __device__ __forceinline__ void gather(u32x4 (&g)[N], rsrc_t rs, u32 voff, u32 soff, Tag tag) {
  u32 lo = 0, hi = 0;
#pragma unroll
  for (int k = 0; k < N; ++k) g[k] = LD16(rs, voff, soff + STEP * k, AUX);
  SB();
#pragma unroll
  for (int k = 0; k < N; ++k) { lo |= (g[k].x ^ tag.lo) | (g[k].z ^ tag.lo); hi |= (g[k].y ^ tag.hi) | (g[k].w ^ tag.hi); }
  if (__builtin_expect(!__all(((lo >> 24) | hi) == 0u), 0)) {
#pragma unroll 1
    for (u32 spin = 0; spin < SPIN_CAP; ++spin) {
      u32 miss = 0;
#pragma unroll
      for (int k = 0; k < N; ++k) if (!__all(ok2(g[k], tag))) miss |= 1u << k;
      if (miss == 0u) break;
      if (SLEEP) __builtin_amdgcn_s_sleep(SLEEP);
      FENCE();
#pragma unroll
      for (int k = 0; k < N; ++k) if (miss >> k & 1u) g[k] = LD16(rs, voff, soff + STEP * k, AUX);
      SB();
    }
  }
}

__global__ void __launch_bounds__(NTHR, 2)
fused_decode(const void*, const void*, const void*, const void*, const void*, const void*, const void*, const void*, void*, void*) {
  extern __shared__ __attribute__((aligned(16))) char lds_raw[];
  LAS char* L = (LAS char*)lds_raw;
  u32 pf; u64x8 ka; u64x2 kb; u64 tt;
  asm volatile("global_load_dword %0, %1, %2" : "=v"(pf) : "v"(min(threadIdx.x * 128u, CODE_SPAN)), "s"(__builtin_amdgcn_s_getpc() & ~(u64)127) : "memory");
  asm volatile("s_load_dwordx16 %0, %3, 0x0\n\ts_load_dwordx4 %1, %3, 0x40\n\ts_memrealtime %2"
               : "=&s"(ka), "=&s"(kb), "=&s"(tt) : "s"(__builtin_amdgcn_kernarg_segment_ptr()) : "memory");
  const int tid = threadIdx.x, lane = tid & 63, wave = __builtin_amdgcn_readfirstlane(tid >> 6);
  const int rb = blockIdx.x, bx = rb & 7, bk = rb >> 3;
  const int h = (bx & 1) + 2 * (bk >> 1), j = (bk & 1) * 4 + (bx >> 1), b = 8 * h + j;
  const int r = lane >> 4, c = lane & 15;
  const bool ffn = b < NFFN, red = rb < NRED;
  const u32 row0 = 80 * j + 20 * wave;
  u32 so_wq = row0 * 8192u + h * 256u, so_gu = row0 * 3456u + 128u * h, so_x = 2u * row0, so_wo = (u32)b * 20480u;
  u32 base = (u32)h * (KV_BYTES / H) + j * 16384u + wave * 4096u;
  u32 vo = 16u * lane, vo_wq = (u32)r * 8192u + 16u * c, vo_gu = (u32)r * 3456u + 16u * (c & 7), vo_x = 4u * lane, vo_r = 2u * r;
  u32 msk[4];
#pragma unroll
  for (int k = 0; k < 4; ++k) msk[k] = (c < 8 && (c >> 1) == k) ? ((c & 1) ? 0xffff0000u : 0x0000ffffu) : 0u;
  asm volatile("" : "+s"(so_wq), "+s"(so_gu), "+s"(so_x), "+s"(so_wo), "+s"(base));
  asm volatile("" : "+v"(vo), "+v"(vo_wq), "+v"(vo_gu), "+v"(vo_x), "+v"(vo_r), "+v"(msk[0]), "+v"(msk[1]), "+v"(msk[2]), "+v"(msk[3]));
  asm volatile("s_waitcnt lgkmcnt(0)" : "+s"(ka), "+s"(kb), "+s"(tt) :: "memory");
  const u32 t0 = (u32)tt;
  const char *px = (const char*)ka[0], *pk = (const char*)ka[1], *pv = (const char*)ka[2], *pwq = (const char*)ka[3], *pwo = (const char*)ka[4],
             *pwg = (const char*)ka[5], *pwu = (const char*)ka[6], *pwd = (const char*)ka[7];
  char *pout = (char*)kb[0], *pws = (char*)kb[1];
  const rsrc_t rws = mkrs(pws, WS_END), rx = mkrs(px, D * 2);
  Tag tag;
  const u32 toff = (u32)h * PER_B / 8192u;
  constexpr u32 TOFF_MAX = 31u * PER_B / 8192u;
  u32 tend = 0;

  if (wave == 4) {
    __builtin_amdgcn_s_setprio(3);
    u32x2 ep = LD8(rws, 0 * lane, WS_EPOCH, SC1);
    if (lane < 4) { FLAG(lane) = 0u; *(volatile LAS u32*)(L + L_LATE + 4 * lane) = 0u; }
    SB();
    {
      asm volatile("" : "+v"(ep));
      asm volatile("" :: "v"(pf));
      u64 z = (((u64)(u32)__builtin_amdgcn_readfirstlane(ep.y) << 32) | (u32)__builtin_amdgcn_readfirstlane(ep.x)) + 0x9E3779B97F4A7C15ull;
      z = (z ^ (z >> 30)) * 0xBF58476D1CE4E5B9ull; z = (z ^ (z >> 27)) * 0x94D049BB133111EBull; z ^= z >> 31;
      tag.hi = (u32)(z >> 32); tag.lo = (u32)z & 0xff000000u;
      if (rb == 0 && lane == 0) ST8((u32x2{(t0 & 0xffffffu) | tag.lo, tag.hi}), rws, 0, WS_T0);
    }
    SB();
    BARRIER();
    { f32x2 s = {0.f, 0.f};
#pragma unroll
      for (int w = 0; w < 4; ++w) s += *(const LAS f32x2*)(L + L_REC + 512 * w + 8 * lane);
      ST16(enc2(s.x, s.y, tag), rws, 16 * lane, WS_QP + (u32)b * 1024u); }
    if (ffn) {
      f32x2 s = {0.f, 0.f};
#pragma unroll
      for (int w = 0; w < 4; ++w) s += *(const LAS f32x2*)(L + L_REC + 2048 + 512 * w + 8 * lane);
      ST16(enc2(s.x, s.y, tag), rws, 16 * lane, WS_GU + (u32)b * 1024u);
    }
    if (rb < 8 && lane < 8) ST8((u32x2{(now() & 0xffffffu) | tag.lo, tag.hi}), rws, 128 * lane, WS_TB + 8 * rb);
    SB();
    u32 tz;
    { until(now() + T_Q);
      u32x2 gt = LD8(rws, 0 * lane, WS_T0, SC1);
      u32x4 g[8];
      gather<8, 1024, 8, SC1>(g, rws, 16 * lane, WS_QP + (u32)h * 8192u, tag);
#pragma unroll 1
      for (u32 spin = 0; !__all(ok1(gt, tag)) && spin < SPIN_CAP; ++spin) { __builtin_amdgcn_s_sleep(8); FENCE(); gt = LD8(rws, 0 * lane, WS_T0, SC1); }
      tz = __builtin_amdgcn_readfirstlane(t0 + (u32)ticks_since(gt.x, t0));
      if (lane == 0) *(LAS u32x4*)(L + L_TAG) = u32x4{tag.hi, tag.lo, tz, 0u};
      float q0 = 0.f, q1 = 0.f;
#pragma unroll
      for (int k = 0; k < 8; ++k) { q0 += dec(g[k].x); q1 += dec(g[k].z); }
      *(LAS u32*)(L + L_Q + 4 * lane) = pack2(q0, q1);
      POST_FLAG(0); }
    SB();
    until(tz + T_START_LOOK);
#pragma unroll 1
    for (u32 spin = 0; spin < 16; ++spin) {
      FENCE();
      const u32x2 gb = LD8(rws, 8 * (lane & 7), WS_TB + 128 * (bk & 7), SC1);
      if (__all(ok1(gb, tag))) {
        float d = (float)ticks_since(gb.x, tz);
        d += dpp<XOR1>(d); d += dpp<XOR2>(d); d += dpp<XOR7>(d);
        const int late = (int)(d * 0.125f) - (int)T_START;
        if (lane == 0 && late > 0) FLAG(2) = (u32)late;
        break;
      }
      __builtin_amdgcn_s_sleep(16);
    }
    if (j < 4 && (lane & 15) < 9 && lane < 32) {
      const u32 t = __builtin_nontemporal_load((const GAS u32*)(((lane & 16) ? pv : pk) + ((u32)h * (KV_BYTES / H) + min((u32)(lane & 15) << 20, KV_BYTES / H - 16384u) + 4096u * j)));
      asm volatile("" :: "v"(t));
    }
    if (red && lane == 0) __builtin_amdgcn_raw_buffer_store_b32(0u, mkrs(pout, D * 2), 0, 2 * RC * rb, SC1);
    const int role = ffn ? (j + h) & 7 : 7;
    if (role == 5) {
      u32x4 g[8];
      gather<8, 1024, 16, SC1_NT>(g, rws, 16 * lane, WS_GU + (u32)h * 8192u, tag);
      float v0 = 0.f, v1 = 0.f;
#pragma unroll
      for (int k = 0; k < 8; ++k) { v0 += dec(g[k].x); v1 += dec(g[k].z); }
      const f32x2 e0 = pair32(rbf(v0)), e1 = pair32(rbf(v1));
      const float h0 = (e0.x > 0.f ? e0.x : e0.x * 0.f) * e0.y, h1 = (e1.x > 0.f ? e1.x : e1.x * 0.f) * e1.y;
      if (lane < 32) ST16(enc2(rbf(h0), rbf(h1), tag), rws, 16 * lane, WS_H + 512u * h);
    } else if (role < 5) {
      const rsrc_t rd = mkrs(pwd, F * D * 2);
      u32x4 wd[16];
      const u32 td = now();
#pragma unroll
      for (int i = 0; i < 16; ++i) {
        if (i && (i & 3) == 0) { SB(); until(td + T_WD * (i >> 2)); SB(); }
        wd[i] = LD16(rd, (u32)r * 1280u + 16u * c, (u32)(64 * h + 4 * i) * 1280u + 256u * role, NTL);
      }
      SB();
      until(td + T_H);
      u32x4 g[1];
      gather<1, 0, 32, SC1>(g, rws, 16 * (lane & 31), WS_H + 512u * h, tag);
      if (lane < 32) *(LAS u32x2*)(L + L_HB + 8 * lane) = u32x2{pack2(dec(g[0].x), dec(g[0].x)), pack2(dec(g[0].z), dec(g[0].z))};
      WAIT_LDS();
      f32x4 da = {0.f, 0.f, 0.f, 0.f};
#pragma unroll
      for (int i = 0; i < 16; ++i) da = rows4(*(const LAS u32*)(L + L_HB + 16 * i + 4 * r), msk, wd[i], da);
      if (lane < 32) {
        const u32 off = (16u * role + c) * 2048u + 32u * r;
        ST16(enc2(da.x, da.y, tag), rws, off, WS_FF + 64u * h); ST16(enc2(da.z, da.w, tag), rws, off + 16u, WS_FF + 64u * h);
      }
    }
    const u32 slip0 = __builtin_amdgcn_readfirstlane(FLAG(2)), tchk = tz + slip0 + due(N_CHK);
    if ((j & 3) == ((h >> 1) & 3)) {
      until(tchk);
      const u32x4 lt = *(const volatile LAS u32x4*)(L + L_LATE);
      if (lane < 8) ST8((u32x2{min(max(max(lt.x, lt.y), max(lt.z, lt.w)), 0xffffu) | tag.lo, tag.hi}), rws, 512 * lane, WS_CK + 8 * (2 * h + (j >> 2)));
    }
    if (red) {
      const u32 xw = LD4(rx, 4 * (lane & 3), 2 * RC * rb, 0);
      while ((int)(now() - (t0 + T_FF)) < 0) __builtin_amdgcn_s_sleep(16);
      u32x4 g0[1], g1[1];
      gather<1, 0, 64, SC1_NT>(g0, rws, 16 * lane, WS_FF + (u32)rb * 2048u, tag);
      gather<1, 0, 64, SC1_NT>(g1, rws, (u32)min(lane >> 2, 10) * 64u + 16u * (lane & 3), WS_FF + (u32)rb * 2048u + 1024u, tag);
      const float f0 = rbf(sum_hi4(dec(g0[0].x) + (lane < 44 ? dec(g1[0].x) : 0.f))), f1 = rbf(sum_hi4(dec(g0[0].z) + (lane < 44 ? dec(g1[0].z) : 0.f)));
      if (lane < 4) *(LAS f32x4*)(L + L_G + 16 * lane) = f32x4{rbf(f0 / (1.f + __expf(-f0))), rbf(f1 / (1.f + __expf(-f1))), f_lo(xw), f_hi(xw)};
    }
    until(tchk + T_CHK_LOOK);
#pragma unroll 1
    for (u32 spin = 0; spin < 4; ++spin) {
      FENCE();
      const u32x2 gc = LD8(rws, 8 * lane, WS_CK + 512 * (bk & 7), SC1);
      if (__all(ok1(gc, tag))) {
        float v = (float)((gc.x & 0xffffffu) * 64u + lane), top = wave_max(v);
#pragma unroll
        for (int k = 1; k < CHK_RANK; ++k) { v = v == top ? 0.f : v; top = wave_max(v); }
        const u32 late = (u32)top >> 6;
        if (lane == 0 && late > T_CHK_MIN) FLAG(2) = slip0 + late;
        break;
      }
      __builtin_amdgcn_s_sleep(16);
    }
    WAIT_FLAG(1, 4u, 16);
    { const f32x4 m4 = *(const LAS f32x4*)(L + L_M);
      const float Mw = fmaxf(fmaxf(m4.x, m4.y), fmaxf(m4.z, m4.w)), Ms = finite_or_0(Mw);
      float l = 0.f;
#pragma unroll 2
      for (int i = 0; i < 16; ++i) { const f32x4 s = *(const LAS f32x4*)(L + L_S + 1024 * i + 16 * lane);
        l += (ex2(s.x - Ms) + ex2(s.y - Ms)) + (ex2(s.z - Ms) + ex2(s.w - Ms)); }
      l = wave_sum(l);
      if (lane == 0) ST16(enc2(Mw, l, tag), rws, 0, WS_ML + 16u * b);
      until(now() + T_ML);
      u32x4 g[1];
      gather<1, 0, 32, SC1>(g, rws, 16 * (lane >> 3), WS_ML + 128u * h, tag);
      const float mj = dec(g[0].x), M = max_hi3(mj), Ls = sum_hi3(ex2(mj - M) * dec(g[0].z));
      const float mw = (lane & 3) == 0 ? m4.x : (lane & 3) == 1 ? m4.y : (lane & 3) == 2 ? m4.z : m4.w;
      if (lane < 4) *(LAS float*)(L + L_F + 4 * lane) = ex2(mw - M) / Ls; }
    POST_FLAG(3);
    {
      const u32 two = tz + __builtin_amdgcn_readfirstlane(FLAG(2)) + due(2 * NT - 1) + TOFF_MAX + T_WO + (31u - h) * T_WO_SPREAD / 32u;
      while ((int)(now() - (two - 100u)) < 0) __builtin_amdgcn_s_sleep(8);
      until(two);
#pragma unroll
      for (int i = 0; i < 20; ++i) dma16(pwo, H * DH * D * 2, L_WO + 1024u * i, vo, so_wo + 1024u * i);
      asm volatile("s_waitcnt vmcnt(0)" ::: "memory"); }
  } else {
#define TOFF(n) (base + (((u32)(n) + 3u * h) & (NT - 1)) * 131072u)
#define ISSUE(slot, rs, n) do { _Pragma("unroll") for (int i_ = 0; i_ < 4; ++i_) ring[slot][i_] = LD16(rs, vo + 1024u * i_, TOFF(n), NTL); SB(); } while (0)
#define WINDOW() asm volatile("s_waitcnt vmcnt(4)" ::: "memory")
    const rsrc_t rk = mkrs(pk, KV_BYTES), rv = mkrs(pv, KV_BYTES);
    u32x4 ring[8][4];
    u32 pt;
    {
      u64 a = (u64)px;
      const u64 a1 = (u64)pwq + so_wq, a2 = (u64)pwg + (ffn ? so_gu : 0u), a3 = (u64)pwu + (ffn ? so_gu : 0u), a4 = (u64)pk + TOFF(0), a5 = (u64)pwo + so_wo;
      a = lane == 1 ? a1 : a; a = lane == 2 ? a2 : a; a = lane == 3 ? a3 : a; a = lane == 4 ? a4 : a; a = lane == 5 ? a5 : a;
      asm volatile("global_load_dword %0, %1, off" : "=v"(pt) : "v"(a) : "memory"); }
    u32 xs[5], xo[5];
#pragma unroll
    for (int i = 0; i < 5; ++i) xs[i] = LD4(rx, vo_x + 256 * i, 0, 0);
#pragma unroll
    for (int i = 0; i < 5; ++i) xo[i] = __builtin_amdgcn_raw_buffer_load_b16(rx, vo_r + 8 * i, so_x, 0);
    SB();
    u32x4 wq[5], gu[5];
    { const rsrc_t rwq = mkrs(pwq, D * H * DH * 2);
#pragma unroll
      for (int i = 0; i < 5; ++i) wq[i] = LD16(rwq, vo_wq, so_wq + 32768u * i, NTL); }
    SB();
    if (ffn) {
      const GAS char* gp = (const GAS char*)(c < 8 ? pwg : pwu) + (so_gu + vo_gu);
#pragma unroll
      for (int i = 0; i < 5; ++i) gu[i] = __builtin_nontemporal_load((const GAS u32x4*)(gp + 13824 * i));
      FENCE();
    } else {
#pragma unroll
      for (int i = 0; i < 5; ++i) gu[i] = u32x4{0u, 0u, 0u, 0u};
    }
    SB();
    {
      float sm = 0.f;
#pragma unroll
      for (int i = 0; i < 5; ++i) sm = dot2(xs[i], 0x3f803f80u, sm);
      const float mean = wave_sum(sm) * (1.f / D);
      float sq = 0.f, sq2 = 0.f;
#pragma unroll
      for (int i = 0; i < 5; ++i) { const float d0 = f_lo(xs[i]) - mean, d1 = f_hi(xs[i]) - mean; sq = fmaf(d0, d0, sq); sq2 = fmaf(d1, d1, sq2); }
      const float var = wave_sum(sq + sq2) * (1.f / D);
      const float mb = rbf(mean), inv = 1.f / rbf(__builtin_amdgcn_sqrtf(rbf(rbf(var) + 1e-5f)));
      u32 xx[5];
#pragma unroll
      for (int i = 0; i < 5; ++i) { const float n = rbf(f_lo(xo[i]) - mb) * inv; xx[i] = pack2(n, n); asm volatile("" : "+v"(xx[i])); }
      SB();
      ISSUE(0, rk, 0); ISSUE(1, rk, 1);
      f32x4 aq = {0.f, 0.f, 0.f, 0.f}, ag = {0.f, 0.f, 0.f, 0.f};
#pragma unroll
      for (int i = 0; i < 5; ++i) { aq = rows4(xx[i], msk, wq[i], aq); ag = rows4(xx[i], msk, gu[i], ag); }
      if (lane < 32) { *(LAS f32x4*)(L + L_REC + 512 * wave + 32 * c + 16 * r) = aq; *(LAS f32x4*)(L + L_REC + 2048 + 512 * wave + 32 * c + 16 * r) = ag; }
      BARRIER(); }
    asm volatile("" :: "v"(pf), "v"(pt));
    SB();
    ISSUE(2, rk, 2);
#pragma unroll
    for (int n = 3; n < 8; ++n) { WINDOW(); until(t0 + toff + due(n)); ISSUE(n, rk, n); }
    WAIT_FLAG(0, 1u, 1);
    const u32x4 q = *(const LAS u32x4*)(L + L_Q + 16 * c);
    u32 tz;
    { const u32x4 tg = *(const LAS u32x4*)(L + L_TAG);
      tag.hi = __builtin_amdgcn_readfirstlane(tg.x); tag.lo = __builtin_amdgcn_readfirstlane(tg.y); tz = __builtin_amdgcn_readfirstlane(tg.z) + toff; }
    const bool b8 = c & 8, b4 = c & 4, b2 = c & 2, b1 = c & 1;
    LAS float* S = (LAS float*)(L + L_S + wave * 4096 + 4 * lane);
    float mrun = -INFINITY;
    u32 slip = 0;
#pragma unroll 1
    for (int n0 = 0; n0 < NT; n0 += 8) {
      const rsrc_t rs = mkrs(n0 + 8 < NT ? pk : pv, KV_BYTES);
      float z[4];
#pragma unroll
      for (int p = 0; p < 8; ++p) {
        float x[4];
#pragma unroll
        for (int i = 0; i < 4; ++i) { float d = 0.f;
#pragma unroll
          for (int k = 0; k < 4; ++k) d = dot2(ring[p][i][k], q[k], d);
          x[i] = d; }
        z[p & 3] = comb<XOR7>(comb<XOR8>(x[0], x[1], b8), comb<XOR8>(x[2], x[3], b8), b4);
        if ((p & 3) == 3) {
          float s = comb<XOR1>(comb<XOR2>(z[0], z[1], b2), comb<XOR2>(z[2], z[3], b2), b1);
          s = rbf(rbf(s) * SCALE);
          mrun = fmaxf(mrun, s);
          S[64 * ((n0 + p) >> 2)] = s;
        } else asm volatile("" : "+v"(z[p & 3]));
        SB();
        WINDOW();
        slip = __builtin_amdgcn_readfirstlane(FLAG(2));
        const u32 late = until_late(tz + slip + due(n0 + p + 8));
        if (n0 < (int)N_CHK - 6 && lane == 0) *(volatile LAS u32*)(L + L_LATE + 4 * wave) = late;
        ISSUE(p, rs, n0 + p + 8);
      }
    }
    const float mw = wave_max(mrun), ms = finite_or_0(mw);
    if (lane == 0) *(LAS float*)(L + L_M + 4 * wave) = mw;
    POST_FLAG(1);
    tz += slip;
    f32x4 acc = {0.f, 0.f, 0.f, 0.f};
    float snext = S[0];
    u32 pp;
#define PV(slot, t, i) acc = rows4(dppu<BCAST + 8 * ((i) & 1) + 4 * ((i) >> 1) + 2 * ((t) & 1) + ((t) >> 1)>(pp), msk, ring[slot][i], acc)
#define VSTEP(n, slot, more) do { PV(slot, (slot) & 3, 0); PV(slot, (slot) & 3, 1); PV(slot, (slot) & 3, 2); PV(slot, (slot) & 3, 3); asm volatile("" : "+v"(acc)); SB(); \
      if (more) { WINDOW(); until(tz + due(NT + (n) + 8)); ISSUE(slot, rv, (n) + 8); } } while (0)
#define VGROUP(n0, s0, more) do { const float pe_ = ex2(snext - ms); pp = pack2(pe_, pe_); snext = S[64 * ((((n0) >> 2) + 1) & 15)]; \
      VSTEP((n0), (s0), more); VSTEP((n0) + 1, (s0) + 1, more); VSTEP((n0) + 2, (s0) + 2, more); VSTEP((n0) + 3, (s0) + 3, more); } while (0)
#pragma unroll 1
    for (int n0 = 0; n0 < NT - 8; n0 += 8) { VGROUP(n0, 0, true); VGROUP(n0 + 4, 4, true); }
    tend = tz + due(2 * NT - 1);
    VGROUP(NT - 8, 0, false); VGROUP(NT - 4, 4, false);
    if (lane < 32) *(LAS f32x4*)(L + L_REC + 512 * wave + 32 * c + 16 * r) = acc;
    POST_FLAG(3);
    if (wave == 0) WAIT_FLAG(3, 5u, 0);
  }
  u32 wo[16];
#define LOAD_WO() do { _Pragma("unroll") for (int k = 0; k < 8; ++k) { \
      const u32 e = *(const LAS u32*)(L + L_WO + 2560 * k + 4 * tid), o = *(const LAS u32*)(L + L_WO + 2560 * k + 1280 + 4 * tid); \
      wo[2 * k] = __builtin_amdgcn_perm(o, e, 0x05040100u); wo[2 * k + 1] = __builtin_amdgcn_perm(o, e, 0x07060302u); } \
    _Pragma("unroll") for (int k = 0; k < 16; ++k) asm volatile("" : "+v"(wo[k])); } while (0)
  if (wave == 0) {
    const f32x4 f4 = *(const LAS f32x4*)(L + L_F);
    f32x2 a = {0.f, 0.f};
#pragma unroll
    for (int w = 0; w < 4; ++w) a += f4[w] * *(const LAS f32x2*)(L + L_REC + 512 * w + 8 * lane);
    ST16(enc2(a.x, a.y, tag), rws, (u32)(lane >> 3) * 1024u + 16u * (lane & 7), WS_IN1 + (u32)h * 8192u + 128u * j);
    SB();
    __builtin_amdgcn_s_sleep(12);
    until_spin(tend + T_HOP1);
    u32x4 g = LD16(rws, 16 * lane, WS_IN1 + (u32)b * 1024u, SC1_NT);
    SB();
    BARRIER();
    LOAD_WO();
    SB();
#pragma unroll 1
    for (u32 spin = 0; !__all(ok2(g, tag)) && spin < SPIN_CAP; ++spin) { __builtin_amdgcn_s_sleep(2); FENCE(); g = LD16(rws, 16 * lane, WS_IN1 + (u32)b * 1024u, SC1_NT); }
    const float o0 = sum_hi3(dec(g.x)), o1 = sum_hi3(dec(g.z));
    if (lane < 8) *(LAS u32*)(L + L_O + 4 * lane) = pack2(o0, o1);
  } else { BARRIER(); LOAD_WO(); }
  BARRIER();
  { const u32x4 oa = *(const LAS u32x4*)(L + L_O), ob = *(const LAS u32x4*)(L + L_O + 16);
    float y0 = 0.f, y1 = 0.f, y2 = 0.f, y3 = 0.f;
#pragma unroll
    for (int k = 0; k < 4; ++k) { y0 = dot2(wo[2 * k], oa[k], y0); y1 = dot2(wo[2 * k + 1], oa[k], y1); y2 = dot2(wo[8 + 2 * k], ob[k], y2); y3 = dot2(wo[9 + 2 * k], ob[k], y3); }
    ST16(enc2(y0 + y2, y1 + y3, tag), rws, (u32)(tid >> 2) * (NWG * 64u) + 16u * (tid & 3), WS_IN2 + 64u * b); }
  if (red) {
    if (wave < 4) {
      __builtin_amdgcn_s_sleep(12);
      until_spin(tend - toff + TOFF_MAX + T_HOP2);
      u32x4 g[4];
      gather<4, 1024, 0, SC1_NT>(g, rws, 16 * lane, WS_IN2 + (u32)rb * (NWG * 64u) + (u32)wave * 4096u, tag);
      const float s0 = sum_hi4((dec(g[0].x) + dec(g[1].x)) + (dec(g[2].x) + dec(g[3].x))), s1 = sum_hi4((dec(g[0].z) + dec(g[1].z)) + (dec(g[2].z) + dec(g[3].z)));
      if (lane < 4) *(LAS f32x2*)(L + L_RS + 32 * wave + 8 * lane) = f32x2{s0, s1};
    }
    BARRIER();
    if (tid < 4) {
      f32x2 s = {0.f, 0.f};
#pragma unroll
      for (int w = 0; w < 4; ++w) s += *(const LAS f32x2*)(L + L_RS + 32 * w + 8 * tid);
      const f32x4 gx = *(const LAS f32x4*)(L + L_G + 16 * tid);
      __builtin_amdgcn_raw_buffer_store_b32(pack2(gx.z + rbf(rbf(s.x) * gx.x), gx.w + rbf(rbf(s.y) * gx.y)), mkrs(pout, D * 2), 4 * tid, 2 * RC * rb, SC1);
      if (rb == 0 && tid == 0) ST8((u32x2{tag.lo, tag.hi}), rws, 0, WS_EPOCH);
    }
  }
}

extern "C" void kernel_launch(void* const* d_in, const int* in_sizes, int n_in, void* d_out, int out_size, void* d_ws, size_t ws_size,
                              hipStream_t stream) {
  (void)in_sizes; (void)n_in; (void)out_size; (void)ws_size;
  (void)hipFuncSetAttribute((const void*)fused_decode, hipFuncAttributeMaxDynamicSharedMemorySize, LDS_BYTES);
  fused_decode<<<NWG, NTHR, LDS_BYTES, stream>>>(d_in[0], d_in[1], d_in[2], d_in[3], d_in[4], d_in[6], d_in[7], d_in[8], d_out, d_ws);
}
```
